```python
import torch

def _softmax(x, axis=-1):
    return torch.softmax(x, dim=axis)
def _gelu(x):
    return torch.nn.functional.gelu(x, approximate="tanh")
def _silu(x):
    return torch.nn.functional.silu(x)
def _sigmoid(x):
    return torch.sigmoid(x)
def _layernorm(x, axis=-1, eps=1e-5):
    m = x.mean(dim=axis, keepdim=True)
    v = x.var(dim=axis, keepdim=True, unbiased=False)
    return (x - m) / torch.sqrt(v + eps)
def _scatter_add(tgt, idx, src, axis=0):
    return tgt.clone().index_add_(axis, idx, src)
def _segment_sum(src, seg, n):
    out = torch.zeros((n,) + src.shape[1:], dtype=src.dtype)
    return out.index_add_(0, seg, src)
def _pad(x, pw):
    flat = tuple(v for pair in reversed(pw) for v in pair)
    return torch.nn.functional.pad(x, flat)

kernel_name = 'sequential_preLN__linear_kernel_hedgehog__mixer_token_mlp__prompt'

def setup_inputs():

    _w = (lambda a: (lambda t: t.to(torch.bfloat16) if t.is_floating_point() else t)(torch.as_tensor(a)))
    def _A_setup():
        B, S, D, H = 1, 2048, 576, 32
        Dh = D // H
        return {
            "x": _w((lambda *s: torch.randn(*s, dtype=torch.bfloat16) * 0.05)(B, S, D)),
            "wqkv": _w((lambda *s: torch.randn(*s, dtype=torch.bfloat16) * 0.2)(D, 3 * D)),
            "w_phi": _w((lambda *s: torch.randn(*s, dtype=torch.bfloat16) * 0.2)(H, Dh, Dh) * (Dh ** -0.5)),
            "wo": _w((lambda *s: torch.randn(*s, dtype=torch.bfloat16) * 0.05)(D, D)),
        }
    _A_in = _A_setup()

    def _B_setup():
        B, S, D, F = 1, 2048, 576, 4608
        return {
            "x": _w((lambda *s: torch.randn(*s, dtype=torch.bfloat16) * 0.05)(B, S, D)),
            "w1": _w((lambda *s: torch.randn(*s, dtype=torch.bfloat16) * 0.4)(S, F) * (S ** -0.5)),
            "b1": _w((lambda *s: torch.randn(*s, dtype=torch.bfloat16) * 12.8)(F) * 0.02),
            "w2": _w((lambda *s: torch.randn(*s, dtype=torch.bfloat16) * 0.4)(F, S) * (F ** -0.5)),
            "b2": _w((lambda *s: torch.randn(*s, dtype=torch.bfloat16) * 6.4)(S) * 0.02),
        }
    _B_in = _B_setup()

    return {
        "A_x": _A_in["x"],
        "A_wqkv": _A_in["wqkv"],
        "A_w_phi": _A_in["w_phi"],
        "A_wo": _A_in["wo"],
        "B_x": _B_in["x"],
        "B_w1": _B_in["w1"],
        "B_b1": _B_in["b1"],
        "B_w2": _B_in["w2"],
        "B_b2": _B_in["b2"],
    }

def reference(A_x, A_wqkv, A_w_phi, A_wo, B_x, B_w1, B_b1, B_w2, B_b2):
    def _A_ref(x, wqkv, w_phi, wo):
        B, S, D = x.shape
        H = 32; Dh = D // H
        qkv = (lambda x, *d: x.permute(*d))((x @ wqkv).reshape(B, S, 3, H, Dh), 2, 0, 3, 1, 4)
        q, k, v = qkv[0], qkv[1], qkv[2]
        pq = torch.einsum("bhsd,hdf->bhsf", q, w_phi)
        pk = torch.einsum("bhsd,hdf->bhsf", k, w_phi)
        phi_q = torch.concatenate([_softmax(pq, axis=-1), _softmax(-pq, axis=-1)], axis=-1)
        phi_k = torch.concatenate([_softmax(pk, axis=-1), _softmax(-pk, axis=-1)], axis=-1)
        kv = torch.einsum("bhsf,bhsd->bhfd", phi_k, v)
        z  = torch.einsum("bhsf->bhf", phi_k)
        num = torch.einsum("bhqf,bhfd->bhqd", phi_q, kv)
        den = torch.einsum("bhqf,bhf->bhq", phi_q, z)[..., None] + 1e-6
        o = num / den
        return (lambda x, *d: x.permute(*d))(o, 0, 2, 1, 3).reshape(B, S, D) @ wo

    def _B_ref(x, w1, b1, w2, b2):
        B, S, D = x.shape
        h = _layernorm(x, axis=-1)
        u = torch.einsum("bsd,sf->bdf", h, w1) + b1
        u = _gelu(u)
        y = torch.einsum("bdf,fs->bds", u, w2) + b2
        return (lambda x, *d: x.permute(*d))(y, 0, 2, 1)

    x = A_x
    h = x + _A_ref(_layernorm(x, axis=-1), A_wqkv, A_w_phi, A_wo)
    h2 = h + _B_ref(_layernorm(h, axis=-1), B_w1, B_b1, B_w2, B_b2)
    return h2
```

```cpp
#include <hip/hip_runtime.h>
#include <hip/hip_bf16.h>
typedef unsigned short u16;
static __device__ __forceinline__ float b2f(u16 v){ return __uint_as_float(((unsigned)v)<<16); }
static __device__ __forceinline__ u16 f2b(float f){ unsigned u=__float_as_uint(f); u += 0x7fffu + ((u>>16)&1u); return (u16)(u>>16); }
constexpr int S=2048, D=576, H=32, DH=18, F=4608;

__global__ void k_ln(const u16* x, float* xn){
  int s=blockIdx.x; __shared__ float red[2][64];
  float a=0,b=0; for(int d=threadIdx.x; d<D; d+=64){ float v=b2f(x[s*D+d]); a+=v; }
  for(int o=32;o;o>>=1) a+=__shfl_xor(a,o);
  float m=a/D;
  for(int d=threadIdx.x; d<D; d+=64){ float v=b2f(x[s*D+d])-m; b+=v*v; }
  for(int o=32;o;o>>=1) b+=__shfl_xor(b,o);
  float r=rsqrtf(b/D+1e-5f);
  for(int d=threadIdx.x; d<D; d+=64) xn[s*D+d]=(b2f(x[s*D+d])-m)*r;
}
__global__ void k_qkv(const float* xn, const u16* w, float* qkv){
  int n=blockIdx.x*64+threadIdx.x; int s=blockIdx.y; if(n>=3*D) return;
  float a=0; for(int k=0;k<D;k++) a+=xn[s*D+k]*b2f(w[k*3*D+n]);
  qkv[s*3*D+n]=a;
}
__global__ __launch_bounds__(64) void k_phi(const float* qkv, const u16* wphi, float* phi){
  int t=blockIdx.x*64+threadIdx.x; if(t>=2*S*H) return;
  int h=t%H, s=(t/H)%S, w=t/(H*S);
  float p[18]; float mx=-1e30f, mn=1e30f;
  #pragma unroll
  for(int f=0;f<18;f++){ float a=0;
    #pragma unroll
    for(int d=0;d<18;d++) a+=qkv[s*3*D+w*D+h*18+d]*b2f(wphi[(h*18+d)*18+f]); p[f]=a; mx=fmaxf(mx,a); mn=fminf(mn,a);}
  float s1=0,s2=0;
  #pragma unroll
  for(int f=0;f<18;f++){ s1+=__expf(p[f]-mx); s2+=__expf(mn-p[f]); }
  #pragma unroll
  for(int f=0;f<18;f++){ phi[(size_t)t*36+f]=__expf(p[f]-mx)/s1; phi[(size_t)t*36+18+f]=__expf(mn-p[f])/s2; }
}
__global__ void k_kv(const float* phi, const float* qkv, float* kv){
  int t=blockIdx.x*64+threadIdx.x; if(t>=H*36*19) return;
  int d=t%19, f=(t/19)%36, h=t/(19*36);
  float a=0; for(int s=0;s<S;s++){ float p=phi[((size_t)(S+s)*H+h)*36+f]; a+= p*(d<18? qkv[s*3*D+2*D+h*18+d]:1.f);} kv[t]=a;
}
__global__ void k_o(const float* phi, const float* kv, float* o){
  int t=blockIdx.x*64+threadIdx.x; if(t>=S*D) return;
  int c=t%D, s=t/D, h=c/18, d=c%18; float n=0,e=0;
  for(int f=0;f<36;f++){ float p=phi[((size_t)s*H+h)*36+f]; n+=p*kv[(h*36+f)*19+d]; e+=p*kv[(h*36+f)*19+18]; }
  o[t]=n/(e+1e-6f);
}
__global__ void k_wo(const float* o, const u16* wo, const u16* x, float* hh){
  int n=blockIdx.x*64+threadIdx.x; int s=blockIdx.y;
  float a=0; for(int k=0;k<D;k++) a+=o[s*D+k]*b2f(wo[k*D+n]);
  hh[s*D+n]=a+b2f(x[s*D+n]);
}
__global__ void k_ln2(const float* h, float* hn){
  int s=blockIdx.x; float a=0,b=0; for(int d=threadIdx.x; d<D; d+=64) a+=h[s*D+d];
  for(int o=32;o;o>>=1) a+=__shfl_xor(a,o);
  float m=a/D;
  for(int d=threadIdx.x; d<D; d+=64){ float v=h[s*D+d]-m; b+=v*v; }
  for(int o=32;o;o>>=1) b+=__shfl_xor(b,o);
  float v=b/D; float r=rsqrtf(v+1e-5f); float r2=rsqrtf(v*r*r+1e-5f);
  for(int d=threadIdx.x; d<D; d+=64) hn[s*D+d]=(h[s*D+d]-m)*r*r2;
}
__global__ void k_g1(const float* hn, const u16* w1, const u16* b1, float* u){
  int f=blockIdx.x*64+threadIdx.x; int d=blockIdx.y;
  float a=0; for(int s=0;s<S;s++) a+=hn[s*D+d]*b2f(w1[(size_t)s*F+f]);
  a+=b2f(b1[f]); float t=0.7978845608f*(a+0.044715f*a*a*a); u[d*F+f]=0.5f*a*(1.f+tanhf(t));
}
__global__ void k_g2(const float* u, const u16* w2, const u16* b2, const float* h, u16* out){
  int s=blockIdx.x*64+threadIdx.x; int d=blockIdx.y;
  float a=0; for(int f=0;f<F;f++) a+=u[d*F+f]*b2f(w2[(size_t)f*S+s]);
  out[s*D+d]=f2b(a+b2f(b2[s])+h[s*D+d]);
}
extern "C" void kernel_launch(void* const* d_in, const int* in_sizes, int n_in, void* d_out, int out_size, void* d_ws, size_t ws_size, hipStream_t stream){
  const u16* x=(const u16*)d_in[0]; const u16* wqkv=(const u16*)d_in[1]; const u16* wphi=(const u16*)d_in[2]; const u16* wo=(const u16*)d_in[3];
  const u16* w1=(const u16*)d_in[5]; const u16* b1=(const u16*)d_in[6]; const u16* w2=(const u16*)d_in[7]; const u16* b2=(const u16*)d_in[8];
  float* ws=(float*)d_ws; float* xn=ws; float* qkv=xn+S*D; float* phi=qkv+S*3*D; float* kv=phi+2*S*H*36; float* o=kv+32768; float* hh=o+S*D; float* hn=hh+S*D; float* u=hn+S*D;
  k_ln<<<S,64,0,stream>>>(x,xn);
  k_qkv<<<dim3(27,S),64,0,stream>>>(xn,wqkv,qkv);
  k_phi<<<2*S*H/64,64,0,stream>>>(qkv,wphi,phi);
  k_kv<<<(H*36*19+63)/64,64,0,stream>>>(phi,qkv,kv);
  k_o<<<S*D/64,64,0,stream>>>(phi,kv,o);
  k_wo<<<dim3(9,S),64,0,stream>>>(o,wo,x,hh);
  k_ln2<<<S,64,0,stream>>>(hh,hn);
  k_g1<<<dim3(F/64,D),64,0,stream>>>(hn,w1,b1,u);
  k_g2<<<dim3(S/64,D),64,0,stream>>>(u,w2,b2,hh,(u16*)d_out);
}
```

```cpp
#include <hip/hip_runtime.h>
#include <hip/hip_bf16.h>
typedef unsigned short u16;
typedef unsigned long long u64;
typedef __attribute__((ext_vector_type(4))) unsigned u32x4;
typedef __attribute__((ext_vector_type(2))) unsigned u32x2;
typedef __attribute__((ext_vector_type(4))) float f32x4;
typedef __attribute__((ext_vector_type(2))) float f32x2;
typedef __attribute__((ext_vector_type(8))) __bf16 bf16x8;
typedef __attribute__((ext_vector_type(4))) __bf16 bf16x4;
typedef __attribute__((ext_vector_type(2))) __bf16 bf16x2;
#define LAS __attribute__((address_space(3)))
#define DI __device__ __forceinline__
constexpr int S=2048, D=576, F=4608;

constexpr unsigned WS_E   = 0;
constexpr unsigned WS_SF  = 4096;
constexpr unsigned WS_SF2 = 12288;
constexpr unsigned WS_GM  = 20480;
constexpr unsigned WS_GR  = WS_GM+16384;
constexpr unsigned WS_GC  = WS_GR+8192;
constexpr unsigned WS_CLR = WS_GC+28672;
constexpr unsigned WS_QF  = WS_CLR;
constexpr unsigned WS_HF  = WS_QF+512*128;
constexpr unsigned WS_UF  = WS_HF+192*128;
constexpr unsigned WS_XF  = WS_UF+216*128;
constexpr unsigned WS_XH  = WS_XF+40*128;
constexpr unsigned WS_SX  = WS_XH+1024*128;
constexpr unsigned WS_KVF = WS_SX+64*12*256;
constexpr unsigned WS_PART= WS_KVF+49152;
constexpr unsigned CLR_KB = (WS_PART+128*11520-WS_CLR)/1024;
static_assert(CLR_KB<=2048&&(WS_PART+128*11520-WS_CLR)%1024==0,"clr");
constexpr unsigned WS_HN  = WS_PART+128*11520;
constexpr unsigned WS_HT  = WS_HN+6*64*6*1024;
constexpr unsigned WS_U   = WS_HT+64*36*1024;
constexpr unsigned WS_X   = WS_U+4*144*9*1024;
constexpr unsigned WS_PQ  = WS_X+64*4*4*9*1024;
constexpr unsigned WS_END = WS_PQ+128*4*9*1024;

DI float b2f(u16 v){ return __uint_as_float(((unsigned)v)<<16); }
DI unsigned pk2(float a,float b){ f32x2 v={a,b}; return __builtin_bit_cast(unsigned,__builtin_convertvector(v,bf16x2)); }
DI float lo2f(unsigned v){ return __uint_as_float(v<<16); }
DI float hi2f(unsigned v){ return __uint_as_float(v&0xffff0000u); }
DI u32x4 mksrd(const void* p, unsigned bytes){ u64 a=(u64)p; u32x4 r={(unsigned)__builtin_amdgcn_readfirstlane((unsigned)a),(unsigned)__builtin_amdgcn_readfirstlane((unsigned)(a>>32))&0xffffu,(unsigned)__builtin_amdgcn_readfirstlane(bytes),0x00020000u}; asm volatile("s_nop 4":"+s"(r)); return r; }
template<int AUX> DI void glds(u32x4 srd, unsigned lds, int voff, int soff){
  if constexpr(AUX==0) asm volatile("s_mov_b32 m0, %0\n\ts_nop 0\n\tbuffer_load_dwordx4 %1, %2, %3 offen lds"::"s"(lds),"v"(voff),"s"(srd),"s"(soff));
  else asm volatile("s_mov_b32 m0, %0\n\ts_nop 0\n\tbuffer_load_dwordx4 %1, %2, %3 offen sc1 lds"::"s"(lds),"v"(voff),"s"(srd),"s"(soff));
}
template<int AUX> DI void glds_lt2(int wid,u32x4 srd, unsigned lds, int voff, int soff){
  if constexpr(AUX==0) asm volatile("s_cmp_lt_u32 %4, 2\n\ts_cbranch_scc0 1f\n\ts_mov_b32 m0, %0\n\ts_nop 0\n\tbuffer_load_dwordx4 %1, %2, %3 offen lds\n1:"::"s"(lds),"v"(voff),"s"(srd),"s"(soff),"s"(wid):"scc");
  else asm volatile("s_cmp_lt_u32 %4, 2\n\ts_cbranch_scc0 1f\n\ts_mov_b32 m0, %0\n\ts_nop 0\n\tbuffer_load_dwordx4 %1, %2, %3 offen sc1 lds\n1:"::"s"(lds),"v"(voff),"s"(srd),"s"(soff),"s"(wid):"scc");
}
#define GL_(v,o,x) "buffer_load_dwordx4 " v ", %1, %2 offen offset:" o x " lds\n\t"
template<int AUX> DI void glds3(u32x4 srd, unsigned lds, int soff, int v0,int v1,int v2){
  if constexpr(AUX==0) asm volatile("s_mov_b32 m0, %0\n\ts_nop 0\n\t" GL_("%3","0","") GL_("%4","1024","") GL_("%5","2048","")::"s"(lds),"s"(srd),"s"(soff),"v"(v0),"v"(v1),"v"(v2));
  else asm volatile("s_mov_b32 m0, %0\n\ts_nop 0\n\t" GL_("%3","0"," sc1") GL_("%4","1024"," sc1") GL_("%5","2048"," sc1")::"s"(lds),"s"(srd),"s"(soff),"v"(v0),"v"(v1),"v"(v2));
}
template<int AUX> DI void glds4(u32x4 srd, unsigned lds, int soff, int v0,int v1,int v2,int v3){
  if constexpr(AUX==0) asm volatile("s_mov_b32 m0, %0\n\ts_nop 0\n\t" GL_("%3","0","") GL_("%4","1024","") GL_("%5","2048","") GL_("%6","3072","")::"s"(lds),"s"(srd),"s"(soff),"v"(v0),"v"(v1),"v"(v2),"v"(v3));
  else asm volatile("s_mov_b32 m0, %0\n\ts_nop 0\n\t" GL_("%3","0"," sc1") GL_("%4","1024"," sc1") GL_("%5","2048"," sc1") GL_("%6","3072"," sc1")::"s"(lds),"s"(srd),"s"(soff),"v"(v0),"v"(v1),"v"(v2),"v"(v3));
}
#define PINS(v) asm volatile("":"+s"(v))
template<int A,int B> DI void waitv2(int wid){
  asm volatile("s_waitcnt vmcnt(%1)\n\ts_cmp_lt_u32 %0, 2\n\ts_cbranch_scc1 1f\n\ts_waitcnt vmcnt(%2)\n1:"::"s"(wid),"n"(A),"n"(B):"memory","scc");
}
template<int N> struct Int{ static constexpr int value=N; };
#define SB() __builtin_amdgcn_sched_barrier(0)
#define WAITV(n) asm volatile("s_waitcnt vmcnt(%0)"::"n"(n):"memory")
#define WAITL0() asm volatile("s_waitcnt lgkmcnt(0)":::"memory")
#define BAR() do{ __builtin_amdgcn_s_barrier(); asm volatile("":::"memory"); }while(0)
#define PIN(v) asm volatile("":"+v"(v))
DI bf16x8 ldsA(const char* p){ return *(const bf16x8*)p; }
DI bf16x4 ldsT(const char* p){ return __builtin_amdgcn_ds_read_tr16_b64_v4bf16((LAS bf16x4*)p); }
DI bf16x8 ldsAi(unsigned a){ return *(const LAS bf16x8*)(size_t)a; }
DI bf16x4 ldsTi(unsigned a){ return __builtin_amdgcn_ds_read_tr16_b64_v4bf16((LAS bf16x4*)(size_t)a); }
DI bf16x8 cat(bf16x4 a,bf16x4 b){ return __builtin_shufflevector(a,b,0,1,2,3,4,5,6,7); }
#define MFMA(a,b,c) __builtin_amdgcn_mfma_f32_16x16x32_bf16(a,b,c,0,0,0)
DI void mfma_ip(f32x4& c, bf16x8 a, bf16x8 b){ asm volatile("v_mfma_f32_16x16x32_bf16 %0, %1, %2, %0":"+v"(c):"v"(a),"v"(b)); }
DI bf16x4 pk4v(f32x4 a){ return __builtin_bit_cast(bf16x4,u32x2{pk2(a[0],a[1]),pk2(a[2],a[3])}); }
DI u32x4 cvt8(f32x4 a,f32x4 b){ return u32x4{pk2(a[0],a[1]),pk2(a[2],a[3]),pk2(b[0],b[1]),pk2(b[2],b[3])}; }
DI bf16x8 asb(u32x4 v){ return __builtin_bit_cast(bf16x8,v); }
template<int CTRL> DI float dppadd(float v){ return v+__builtin_bit_cast(float,__builtin_amdgcn_update_dpp(0,__builtin_bit_cast(int,v),CTRL,0xf,0xf,true)); }
template<int CTRL> DI float dppmov(float v){ return __builtin_bit_cast(float,__builtin_amdgcn_update_dpp(0,__builtin_bit_cast(int,v),CTRL,0xf,0xf,true)); }
DI float row8sum(float v){ v=dppadd<0xB1>(v); v=dppadd<0x4E>(v); v=dppadd<0x141>(v); return v; }
DI float dot2(unsigned a, unsigned b, float c){ return __builtin_amdgcn_fdot2_f32_bf16(__builtin_bit_cast(bf16x2,a),__builtin_bit_cast(bf16x2,b),c,false); }
DI unsigned hilo(float v){ unsigned h=pk2(v,0.f)&0xffffu; float r=v-lo2f(h); return h|(pk2(r,0.f)<<16); }
typedef __amdgpu_buffer_rsrc_t rsrc_t;
DI unsigned ld32(rsrc_t r,unsigned v,unsigned s){ return __builtin_amdgcn_raw_buffer_load_b32(r,v,s,16); }
DI u32x2 ld64(rsrc_t r,unsigned v,unsigned s){ return __builtin_amdgcn_raw_buffer_load_b64(r,v,s,16); }
DI u32x4 ld128(rsrc_t r,unsigned v,unsigned s){ return __builtin_amdgcn_raw_buffer_load_b128(r,v,s,16); }
DI void st32(rsrc_t r,unsigned x,unsigned v,unsigned s){ __builtin_amdgcn_raw_buffer_store_b32(x,r,v,s,16); }
DI void st64(rsrc_t r,u32x2 x,unsigned v,unsigned s){ __builtin_amdgcn_raw_buffer_store_b64(x,r,v,s,16); }
DI void st128(rsrc_t r,u32x4 x,unsigned v,unsigned s){ __builtin_amdgcn_raw_buffer_store_b128(x,r,v+s,0,16); }
constexpr unsigned OOB=0x80000000u;
constexpr unsigned SENT=0xFFFFFFFFu;
DI unsigned nos(unsigned v){ return v<SENT-1u?v:SENT-1u; }
DI u32x4 nos4(u32x4 v){ return u32x4{nos(v[0]),nos(v[1]),nos(v[2]),nos(v[3])}; }
DI unsigned mx4(u32x4 v){ unsigned a=v[0]>v[1]?v[0]:v[1], b=v[2]>v[3]?v[2]:v[3]; return a>b?a:b; }
DI unsigned mxu(unsigned a,unsigned b){ return a>b?a:b; }
#define CFENCE() asm volatile("":::"memory")
constexpr unsigned SPIN=1u<<21;

DI unsigned gelu2(float a0,float a1){
  const f32x2 a={a0,a1};
  f32x2 p=(a*a)*(-0.044715f*2.f*0.7978845608f*1.44269504f)+(-2.f*0.7978845608f*1.44269504f);
  p=p*a;
  f32x2 e={__builtin_amdgcn_exp2f(p[0]),__builtin_amdgcn_exp2f(p[1])};
  e=e+1.f;
  f32x2 r={__builtin_amdgcn_rcpf(e[0]),__builtin_amdgcn_rcpf(e[1])};
  r=r*a;
  return pk2(r[0],r[1]);
}
DI void feat(const f32x4 (&p)[5], float sc, unsigned (&o)[18]){
  float v[18];
  #pragma unroll
  for(int f=0;f<18;f++) v[f]=p[f>>2][f&3];
  float mx=fmaxf(v[0],v[17]),mn=fminf(v[0],v[17]);
  #pragma unroll
  for(int f=1;f<17;f+=2){ mx=fmaxf(fmaxf(mx,v[f]),v[f+1]); mn=fminf(fminf(mn,v[f]),v[f+1]); }
  const float a=-mx*sc, b=mn*sc;
  f32x2 e[9],n[9],se,sn;
  #pragma unroll
  for(int f=0;f<9;f++){
    f32x2 x={v[2*f],v[2*f+1]}; f32x2 y=x*sc+a, z=b-x*sc;
    e[f]=f32x2{__builtin_amdgcn_exp2f(y[0]),__builtin_amdgcn_exp2f(y[1])};
    n[f]=f32x2{__builtin_amdgcn_exp2f(z[0]),__builtin_amdgcn_exp2f(z[1])};
    if(f){ se+=e[f]; sn+=n[f]; } else { se=e[0]; sn=n[0]; } }
  const float re=__builtin_amdgcn_rcpf(se[0]+se[1]), rn=__builtin_amdgcn_rcpf(sn[0]+sn[1]);
  #pragma unroll
  for(int f=0;f<9;f++){ f32x2 u=e[f]*re, w=n[f]*rn; o[f]=pk2(u[0],u[1]); o[9+f]=pk2(w[0],w[1]); }
}

#define H1 2
#define HOP1SLEEP 2
#define S2SLEEP 1
#define P3A_T kperm(t,(mt+nt)&3,32)
#define P3B_T kperm(t,(mt+nt)&3,32)
#define OAUX 16
#define S2INIT 10
#define CPRIO 1
#define PF1B 5
#define LATE 500
#define PF2N 4
#define LPRIO 3
#define CAP 11
#define P4W 3
#define SL5 1
#define S3INIT 10
#define S3EXT 10
#define KINIT 10
#define QSKIP 1
#define PF1A 1
#define HOP2SLEEP 2
#define SL1 1
#define SL0 6

DI int kperm(int t,int r,int NT){ if(t==0) return r; const int n=t-1, m=NT-1-((NT-1-r)&3); return n>=m? n+1 : n+(((n&3)==r)?4:0); }
template<int H> DI int kpermh(int t,int r,int NT){ return t<H? t : H+kperm(t-H,r,NT-H); }
constexpr int LDS_BYTES=163840;
constexpr int P1_STB=34816, P1_M=139264;
constexpr int P1_PH=104448, P1_VT=141312, P1_KB=81920;
constexpr int P2_STB=24576, P2_O=122880, P2_FS=160768;
constexpr int P3_STB=28672, P3_ST=5;
constexpr int P4_STB=34816, P4_H=139264;

constexpr int CODE_BYTES=42600;

__global__ __launch_bounds__(512,2) void fused(const u16* __restrict__ x, const u16* __restrict__ wqkv, const u16* __restrict__ wphi, const u16* __restrict__ wo,
    const u16* __restrict__ w1, const u16* __restrict__ b1, const u16* __restrict__ w2, const u16* __restrict__ b2, u16* __restrict__ out, char* __restrict__ ws){
  extern __shared__ __attribute__((aligned(1024))) char sm[];
  int wid=__builtin_amdgcn_readfirstlane(threadIdx.x>>6); const int lane=threadIdx.x&63;
  asm volatile("s_nop 4":"+s"(wid));
  const int b=blockIdx.x, xc=b&7, ii=b>>3;
  const bool ldr=wid<4; const int lw=wid&3;
  const unsigned smb=(unsigned)(size_t)(LAS char*)sm;
  const int g=lane>>4,q4=(lane>>2)&3,p=lane&3,c=lane&15;
  u64 pc0; asm volatile("s_getpc_b64 %0":"=s"(pc0));
  auto codepf=[&](int chunk){
    const u32x4 rc=mksrd((const void*)(pc0&~127ull),CODE_BYTES);
    asm volatile("s_mov_b32 m0, %0\n\ts_nop 0\n\tbuffer_load_dword %1, %2, %3 offen lds"::"s"(smb+163328),"v"(lane*128),"s"(rc),"s"(chunk*8192)); };
  auto tlbw=[&](){
    if(ii==24&&wid<7){
      const char* bp=wid==0?ws:(wid==1?(const char*)w1:(wid==2?(const char*)w2:(wid==3?(const char*)out:(wid==4?(const char*)b1:(wid==5?(const char*)b2:(const char*)wo)))));
      const unsigned sz=wid==0?WS_END:(wid<3?S*F*2:(wid==3?S*D*2:(wid==4?F*2:(wid==5?S*2:D*D*2))));
      const u32x4 rt=mksrd(bp,sz);
      asm volatile("s_mov_b32 m0, %0\n\ts_nop 0\n\tbuffer_load_dword %1, %2, 0 offen lds"::"s"(smb+163328),"v"(lane*524288+1024),"s"(rt));
    } };
  const int cj=(ii-28)*4+(xc>>1), cn=(xc&1)?4:5, cd=(xc&1)?cj>>2:(cj*13)>>6, slab=cd*9+(xc&1)*5+cj-cd*cn;
  if(ii>=24){
    if(wid==7) codepf(ii-24);
    const bool cs=ii>=28;
    const u32x4 rs=mksrd(cs? (const char*)wqkv+slab*128 : (const char*)x+(xc*4+ii-24)*64*1152, cs? (cj<3*cn?576*3456-slab*128:0) : 64*1152);
    const int vo_=(threadIdx.x>>3)*(cs?3456:1152)+((lane&7)^(2*g))*16;
    #pragma nounroll
    for(int i=0;i<9;i++) glds<0>(rs,smb+16384+(i*8+wid)*1024,vo_,i*(cs?64*3456:128));
  }
  SB();
  const bool isq=ii>=16;
  const int tt0=4*(xc>>1)+(ii&3);
  const int hg=isq? 2*(xc&1)+((ii-16)>>2) : 4*(xc&1)+(ii>>2);
  u32x4 rX,rW; int voX[4],voW[5];
  const int cbase=isq? 288*hg : 1152+144*hg;
  auto ldx=[&](int t_,int slot){ const int kt=kpermh<H1>(t_,((ii>>2)+ii)&3,9);
    glds4<0>(rX,smb+slot*P1_STB+lw*4096,kt*128,voX[0],voX[1],voX[2],voX[3]);
  };
  auto ldw=[&](int t_,int slot){ const int kt=kpermh<H1>(t_,((ii>>2)+ii)&3,9);
    glds4<0>(rW,smb+slot*P1_STB+16384+lw*4096,kt*64*3456,voW[0],voW[1],voW[2],voW[3]);
    glds_lt2<0>(lw,rW,smb+slot*P1_STB+16384+(16+lw)*1024,voW[4],kt*64*3456);
  };
  unsigned val[6];
  const rsrc_t R=__builtin_amdgcn_make_buffer_rsrc(ws,0,WS_END,0x00020000);
  u32x2 ev={0,0};
  if(__builtin_expect(ii<24,1)){
    if(ldr){
      __builtin_amdgcn_s_setprio(3);
      rX=mksrd((const char*)x+(size_t)tt0*128*1152, 128*1152);
      #pragma unroll
      for(int q=0;q<4;q++){ int row=lane>>3; voX[q]=((lw*4+q)*8+row)*1152-q*1024+(((lane&7)^((4*(q&1)+(row>>1))&7))*16); }
      ldx(0,0);
      SB();
      const int cadd=isq?0:1008;
      rW=mksrd((const char*)wqkv+cbase, 576*3456-cbase);
      #pragma unroll
      for(int q=0;q<5;q++){ int id=(q<4?lw*4+q:16+lw)*64+lane; int rho=(id*3641)>>16, ch=id-rho*18;
        int k=(rho&35)+2*(rho&12)+((rho>>2)&4);
        voW[q]=k*3456+16*ch+(ch>=9?cadd:0)-(q<4?q*1024:0); }
      ldw(0,0);
      ldx(1,1); ldw(1,1);
      int tkt=kpermh<H1>(lane,((ii>>2)+ii)&3,9); PIN(tkt);
      #pragma nounroll
      for(int n=2;n<9;n++){
        const int kt=__builtin_amdgcn_readlane(tkt,n);
        int sx=kt*128, sw=kt*64*3456; unsigned lx=smb+(n&3)*P1_STB+lw*4096, lv=lx+16384, lz=lx+32768-lw*3072;
        PINS(sx); PINS(sw); PINS(lx); PINS(lv); PINS(lz);
        waitv2<9,8>(lw);
        BAR();
        glds4<0>(rW,lv,sw,voW[0],voW[1],voW[2],voW[3]); glds4<0>(rX,lx,sx,voX[0],voX[1],voX[2],voX[3]); glds_lt2<0>(lw,rW,lz,voW[4],sw);
      }
    } else {
      const rsrc_t rp=__builtin_amdgcn_make_buffer_rsrc((void*)((const unsigned*)wphi+(hg*4+lw)*(isq?324:162)),0,isq?1296:648,0x00020000);
      #pragma unroll
      for(int i=0;i<6;i++) val[i]=__builtin_amdgcn_raw_buffer_load_b32(rp,lane*4+256*i,0,0);
    }
  } else {
    const int tid=threadIdx.x;
    ev=ld64(R,0,WS_E);
    WAITV(0);
    if(ii>=28){
      BAR();
      if(wid<4&&cj<3*cn){
        const char* sp=sm+16384+(4*g+q4)*128+((wid^(((4*g+q4)>>1)&3))*32)+8*p;
        bf16x4 w[36];
        #pragma unroll
        for(int i=0;i<36;i++) w[i]=ldsT(sp+i*2048);
        SB();
        f32x4 a={0,0,0,0};
        #pragma unroll
        for(int i=0;i<18;i++) a=MFMA(asb(u32x4{0x3f803f80u,0x3f803f80u,0x3f803f80u,0x3f803f80u}),cat(w[2*i],w[2*i+1]),a);
        const unsigned h=hilo(a[0]);
        st64(R,u32x2{h,h},g==0? c*16+256 : ((g==1&&(slab|wid))? c*16+8 : OOB),WS_GC+slab*1024+wid*256-256);
      }
    } else {
      u32x4 v[9];
      #pragma unroll
      for(int i=0;i<9;i++) v[i]=*(const u32x4*)(sm+16384+(i*8+wid)*1024+lane*16);
      const int row=(xc*4+ii-24)*64+(tid>>3);
      float a[2]={0,0},q[2]={0,0};
      #pragma unroll
      for(int i=0;i<9;i++)
        #pragma unroll
        for(int j=0;j<4;j++){ a[j&1]=dot2(v[i][j],0x3f803f80u,a[j&1]); q[j&1]=dot2(v[i][j],v[i][j],q[j&1]); }
      const float mu=row8sum(a[0]+a[1])*(1.f/576.f);
      const float rstd=__builtin_amdgcn_rsqf(fmaxf(row8sum(q[0]+q[1])*(1.f/576.f)-mu*mu,0.f)+1e-5f);
      { const unsigned h=hilo(-mu); st64(R,u32x2{(h&0xffffu)|(h<<16),(h>>16)|(h&0xffff0000u)},(tid&7)?OOB:row*8,WS_GM); }
      st32(R,__float_as_uint(rstd),(tid&7)?OOB:row*4,WS_GR);
    }
    __builtin_amdgcn_s_waitcnt(0x0F70);
  }
  SB();
  const int vi=ii<16?ii:(ii<24?ii+8:ii-8);
  const int L3=xc*27+vi, nt=L3/6, mt=L3-nt*6;
  const int kq=xc>>1, mb=ii&3, nb=(xc&1)*8+(ii>>2), tile=mb*16+nb;
  const u32x4 sent4={SENT,SENT,SENT,SENT};
  auto set_flag=[&](unsigned so_){ st32(R,1,lane==0?0:OOB,so_); };
  auto wait_flags=[&](unsigned vo_,unsigned so_){
    int any=0;
    #pragma nounroll
    for(unsigned sp=0;sp<SPIN;sp++){ CFENCE();
      unsigned v=ld32(R,vo_,so_);
      if(__all(v==1u||vo_==OOB)) break;
      any|=__any(v==1u);
      if(v==1u) vo_=OOB;
      if(any) __builtin_amdgcn_s_sleep(SL1); else __builtin_amdgcn_s_sleep(SL0);
    }
    CFENCE();
  };
  auto wait_mask=[&](unsigned vo_,unsigned so_,u64 need){
    u64 m=0;
    #pragma nounroll
    for(unsigned sp=0;sp<SPIN;sp++){ CFENCE();
      m|=__ballot(ld32(R,vo_,so_)==1u);
      if((m&need)==need) break;
      if(m) __builtin_amdgcn_s_sleep(SL1); else __builtin_amdgcn_s_sleep(SL0);
    }
    CFENCE();
    return m|need;
  };
  auto mktag=[&](){ return u32x2{ev[0]^0x7F4A7C15u,ev[1]^0x9E3779B9u}; };
  auto check_tags=[&](unsigned so_,bool rd){
    #pragma nounroll
    for(unsigned sp=0;sp<SPIN;sp++){ CFENCE(); if(rd) ev=ld64(R,0,WS_E); const u32x2 v=ld64(R,lane*128,so_), tag=mktag(); if(__all(v[0]==tag[0]&&v[1]==tag[1])) break; __builtin_amdgcn_s_sleep(2); }
    CFENCE();
  };

  auto vb3=[&](int q){ return (lw*16+q*4+g)*(F*2)-q*1024+(((c>>1)^(4*(q&1)+g))*32)+(c&1)*16; };
  auto ldb3=[&](int t,int slot){
    glds4<0>(mksrd((const char*)w1+nt*256, S*F*2-nt*256),smb+slot*P3_STB+lw*4096,(P3B_T)*64*(F*2),vb3(0),vb3(1),vb3(2),vb3(3));
  };
  const int tb=8*xc+vi/3, cb=vi%3;
  int vo[3]; u32x4 xa[2][2];
  auto calcvo=[&](){
    #pragma unroll
    for(int v=0;v<3;v++){ int id=64*v+lane; int row=(id*171)>>12, pos=id-row*24; vo[v]=(lw*8+row)*1152-v*1024+(((pos>>1)^((row>>1)&3))*32)+(pos&1)*16; } };
  auto ldwo=[&](int t,int slot){
    const u32x4 rWo=mksrd((const char*)wo+cb*384, 576*1152-cb*384);
    const unsigned dst=smb+slot*P2_STB;
    glds3<0>(rWo,dst+lw*3072,t*64*1152,vo[0],vo[1],vo[2]); glds3<0>(rWo,dst+lw*3072+12288,(t*64+32)*1152,vo[0],vo[1],vo[2]);
  };
  auto ldxa=[&](){
    #pragma unroll
    for(int t=0;t<2;t++)
      #pragma unroll
      for(int u=0;u<2;u++) xa[t][u]=*(const u32x4*)((const char*)x+(size_t)(tb*32+16*t+c)*1152+cb*384+lw*96+u*32+g*16); };
  unsigned pacc=0; const int pt=threadIdx.x+(ii&7)*512;
  auto pf1=[&](int i){ int id=pt+i*4096; int row=(id*52429)>>19, cc=id-row*10; pacc^=*(const unsigned*)((const char*)w1+(size_t)row*F*2+(xc*27/6)*256+cc*128); };
  if(__builtin_expect(ii>=24,0)){
    const int sid=b-192;
    auto clr=[&](){
      #pragma unroll
      for(int i=0;i<4;i++){ const unsigned k=sid*8+wid+512*i; st128(R,sent4,k<CLR_KB?lane*16:OOB,WS_CLR+k*1024); } };
    WAITV(0); BAR();
    if(wid==0) st64(R,mktag(),lane<16?lane*8:OOB,WS_SF+sid*128);
    clr();
    WAITV(0); BAR();
    if(wid==0) st64(R,mktag(),lane<16?lane*8:OOB,WS_SF2+sid*128);
    tlbw();
    #pragma unroll
    for(int i=0;i<2;i++){ int id=pt+i*4096; pacc^=*(const unsigned*)((const char*)wo+(id<5184?id:0)*128); }
    #pragma unroll
    for(int i=0;i<PF1A;i++) pf1(i);
    asm volatile(""::"v"(pacc));
    if(wid==0) check_tags(WS_SF2,false);
    if(wid==7) codepf(ii-24);
    if(ldr){ calcvo(); ldwo(0,0); ldwo(1,1); ldwo(2,2); } else ldxa();
    WAITV(0); BAR();
  } else {
    bf16x4 B[2][5]; float sc0,sc1;
    const bf16x4 z4=__builtin_bit_cast(bf16x4,u32x2{0,0});
    const bool q1=isq&&ldr;
    const char* mbp=sm+P1_M+(4*g+q4)*160+8*p;
    const char* mba=mbp+(q1?80*160:0);
    bf16x4 A[5][5];
    const int kbo=P1_KB+lw*(isq?5632:2816)+lane*8;
    if(__builtin_expect(ldr,1)){
      waitv2<9,8>(lw); BAR();
      if(lw==0) check_tags(WS_SF,true);
      calcvo();
      __builtin_amdgcn_s_waitcnt(0x0F70); WAITV(0); BAR();
      if(lw==0) check_tags(WS_SF2,false);
      #pragma unroll
      for(int o=0;o<5;o++){
        #pragma unroll
        for(int k=0;k<4;k++) A[o][k]=ldsT(mba+k*16*160+32*o);
        A[o][4]=ldsT(mbp+64*160+32*o); }
      SB();
      if(!isq) ldwo(2,2);
      __builtin_amdgcn_s_setprio(0);
      WAITL0(); BAR();
      #pragma unroll
      for(int j=0;j<5;j++){ B[0][j]=*(const bf16x4*)(sm+kbo+512*j); B[1][j]=*(const bf16x4*)(sm+kbo+(isq?2816:0)+512*j); }
      sc0=*(const float*)(sm+kbo-lane*4+2560); sc1=*(const float*)(sm+kbo-lane*4+2560+(isq?2816:0));
    } else {
      unsigned msc[6];
      {
        const int Rr=isq?36:18, r0=Rr*lw, nd=Rr*9;
        const int nch=(Rr+((!isq&&lw==3)?8:0))*10;
        #pragma unroll
        for(int i=0;i<6;i++){ int id=lane+64*i; *(u32x4*)(sm+(id<nch? P1_M+r0*160+id*16 : 162304+lane*16))=u32x4{0,0,0,0}; }
        #pragma unroll
        for(int i=0;i<6;i++){ int id=lane+64*i; int row=(id*57)>>9, fp=id-row*9; int hl=(isq?2*lw+(row>=18?1:0):lw)&3;
          msc[i]=smb+(id<nd? P1_M+(r0+row)*160+32*(fp>>1)+8*hl+4*(fp&1) : 163584+lane*4); PIN(msc[i]); }
      }
        f32x4 acc[2][9];
      #pragma unroll
      for(int t=0;t<2;t++)
        #pragma unroll
        for(int j=0;j<9;j++) acc[t][j]=f32x4{0,0,0,0};
      int adX=(32*lw+c)*128+((g^((c>>1)&7))*16);
      int adW=16384+(4*g+q4)*288+8*p;
      bf16x8 x0[2],x1[2]; bf16x4 w0[18],w1f[18];
      #pragma unroll
      for(int i=0;i<2;i++) x1[i]=asb(u32x4{0,0,0,0});
      #pragma unroll
      for(int j=0;j<18;j++) w1f[j]=z4;
      u32x2 gm[2]; unsigned gr[2]; u32x4 gc[5];
      auto mm=[&](int m, bf16x4 (&wc)[18], bf16x8 (&xc_)[2]){ const int j=m>>1,t=m&1;
        acc[t][j]=MFMA(cat(wc[2*j],wc[2*j+1]),xc_[t],acc[t][j]);
      };
      auto half=[&](bf16x4 (&wc)[18], bf16x8 (&xc_)[2], bf16x4 (&wx)[18], bf16x8 (&xx)[2], const char* sb, int ks){
        #pragma unroll
        for(int m=0;m<18;m++){
          mm(m,wc,xc_);
          SB();
          if(m<2) xx[m]=ldsA(sb+(adX^(ks*64))+m*2048);
          wx[m]=ldsT(sb+adW+(32*ks+16*(m&1))*288+32*(m>>1));
          SB();
        }
      };
      #pragma unroll
      for(int j=0;j<9;j++){ PIN(acc[0][j]); PIN(acc[1][j]); PIN(w1f[2*j]); PIN(w1f[2*j+1]); }
      PIN(x1[0]); PIN(x1[1]); PIN(adX); PIN(adW);
      CFENCE(); SB();
      #pragma unroll
      for(int i=0;i<6;i++) *(LAS unsigned*)(size_t)msc[i]=val[i];
      #pragma nounroll
      for(int t=0;t<8;t++){
        BAR();
        const char* sb=sm+(t&3)*P1_STB;
        SB();
        half(w1f,x1,w0,x0,sb,0);
        half(w0,x0,w1f,x1,sb,1);
      }
      BAR();
      { const int t0=tt0*128+32*lw, cq=(isq?144*hg:576+72*hg)*16, cv=isq?0:504*16;
        #pragma unroll
        for(int u=0;u<2;u++) gm[u]=ld64(R,g?OOB:c*8,WS_GM+(t0+16*u)*8);
        #pragma unroll
        for(int j=0;j<5;j++) gc[j]=ld128(R,c*16+((j==2&&c>=8)?cv:0),WS_GC+cq+(j<3?512*j:512*j-256+cv));
        #pragma unroll
        for(int u=0;u<2;u++) gr[u]=ld32(R,c*4,WS_GR+(t0+16*u)*4); }
      SB();
      half(w1f,x1,w0,x0,sm,0);
      half(w0,x0,w1f,x1,sm,1);
      #pragma unroll
      for(int m=0;m<18;m++){ mm(m,w1f,x1); SB();
        if(m<15){ const int o=m/3,k=m%3; if(k<2){ A[o][2*k]=ldsT(mba+2*k*16*160+32*o); A[o][2*k+1]=ldsT(mba+(2*k+1)*16*160+32*o);} else A[o][4]=ldsT(mbp+64*160+32*o); }
        SB(); }
      __builtin_amdgcn_s_waitcnt(0x0F70);
      {
        bf16x8 ml[2],mh[2];
        #pragma unroll
        for(int t=0;t<2;t++){ ml[t]=asb(u32x4{gm[t][0],gm[t][1],0u,0u}); mh[t]=asb(u32x4{0u,0u,gm[t][0],gm[t][1]}); }
        #pragma unroll
        for(int t=1;t>=0;t--)
          #pragma unroll
          for(int j=0;j<9;j++){ const int jj=j<5?j:j+1; acc[t][j]=MFMA(asb(gc[jj>>1]),(jj&1)?mh[t]:ml[t],acc[t][j]); }
      }
      sc0=__uint_as_float(gr[0]); sc1=__uint_as_float(gr[1]);
      __builtin_amdgcn_s_setprio(CPRIO);
      if(isq){
        #pragma unroll
        for(int t=0;t<2;t++){ char* d=sm+kbo+2816*t;
          #pragma unroll
          for(int j=0;j<4;j++) *(bf16x4*)(d+512*j)=pk4v(acc[t][5+j]);
          bf16x4 u=pk4v(acc[t][4]);
          *(bf16x4*)(d+2048)=g>=2?u:z4; B[t][4]=g<2?u:z4;
          *(float*)(d-lane*4+2560)=t?sc1:sc0; }
        WAITL0(); BAR();
        #pragma unroll
        for(int t=0;t<2;t++)
          #pragma unroll
          for(int j=0;j<4;j++) B[t][j]=pk4v(acc[t][j]);
      } else {
        char* d=sm+kbo;
        #pragma unroll
        for(int j=0;j<4;j++) *(bf16x4*)(d+512*j)=pk4v(acc[1][j]);
        *(bf16x4*)(d+2048)=g<2?pk4v(acc[1][4]):z4;
        *(float*)(d-lane*4+2560)=sc1;
        WAITL0(); BAR();
        char* dv=sm+P1_VT+(32*lw+c)*160+8*g;
        #pragma unroll
        for(int j=0;j<5;j++){ *(bf16x4*)(dv+32*j)=pk4v(acc[0][4+j]*sc0); *(bf16x4*)(dv+16*160+32*j)=pk4v(acc[1][4+j]*sc1); }
        #pragma unroll
        for(int j=0;j<4;j++){ B[0][j]=pk4v(acc[0][j]); B[1][j]=z4; }
        B[0][4]=g<2?pk4v(acc[0][4]):z4; B[1][4]=z4;
      }
    }
    {
      const int hq=(ldr?4:0)+g;
      unsigned dq=WS_PQ+((tt0*8+2*lw)*4+hg)*9216;
      #pragma nounroll
      for(int t=0;t<2;t++){
        f32x4 pp[5];
        #pragma unroll
        for(int o=0;o<5;o++) pp[o]=MFMA(cat(A[o][4],z4),cat(B[0][4],z4),(f32x4{0,0,0,0}));
        #pragma unroll
        for(int o=0;o<5;o++) pp[o]=MFMA(cat(A[o][0],A[o][1]),cat(B[0][0],B[0][1]),pp[o]);
        #pragma unroll
        for(int o=0;o<5;o++) pp[o]=MFMA(cat(A[o][2],A[o][3]),cat(B[0][2],B[0][3]),pp[o]);
        SB(); __builtin_amdgcn_s_setprio(0);
        if(ldr&&!isq){ ldwo(0,0); ldwo(1,1); }
        SB();
        unsigned ph[18]; feat(pp,sc0*1.44269504f,ph);
        if(!isq){
          char* dst=sm+P1_PH+(32*lw+(ldr?16:0)+c)*288+72*g;
          #pragma unroll
          for(int i=0;i<9;i++) *(u32x2*)(dst+8*i)=u32x2{ph[2*i],ph[2*i+1]};
          break;
        }
        #pragma unroll
        for(int i=0;i<4;i++) st128(R,u32x4{ph[4*i],ph[4*i+1],ph[4*i+2],ph[4*i+3]},hq*1024+(i*16+c)*16,dq);
        st64(R,u32x2{ph[16],ph[17]},8192+((hq>>1)*16+c)*16+(hq&1)*8,dq);
        dq+=36864;
        #pragma unroll
        for(int j=0;j<5;j++) B[0][j]=B[1][j];
        sc0=sc1;
        if(!ldr) __builtin_amdgcn_s_setprio(CPRIO);
      }
    }
    __builtin_amdgcn_s_setprio(0);
    if(isq){ WAITV(0); set_flag(WS_QF+((tt0*4+hg)*8+wid)*128); }
    else {
      if(!ldr) ldxa();
      const int h3=wid>>1, ct=wid&1, hb=h3&1;
      const char* s3p=sm+P1_PH+(4*g+q4)*288+72*h3+8*p; const char* s3v=sm+P1_VT+(4*g+q4)*160+8*p+(ct? 48+72*(h3>>1) : 16+36*h3+4*hb);
      const bool okc= ct? (c==4+hb||(c>>1)==hb) : true;
      unsigned ad3[3];
      #pragma unroll
      for(int ft=0;ft<3;ft++){ const int fq=4*ft+g;
        const int id= ft<2? (ct? 256+(fq>>1)*16 : hb*128+(fq>>1)*32)+2*c+(fq&1) : (ct?352+c:320+hb+2*c);
        ad3[ft]=(okc&&(ft<2||g==0))? WS_PART+(hg*16+tt0)*11520+((h3>>1)*360+id)*16 : OOB; PIN(ad3[ft]); }
      WAITL0(); BAR();
      {
        bf16x4 ph[4][3][2]; u32x2 vf[4][2];
        #pragma unroll
        for(int w=0;w<4;w++)
          #pragma unroll
          for(int e2=0;e2<2;e2++){ vf[w][e2]=__builtin_bit_cast(u32x2,ldsT(s3v+(32*w+16*e2)*160));
            #pragma unroll
            for(int ft=0;ft<3;ft++) ph[w][ft][e2]=ldsT(s3p+(32*w+16*e2)*288+32*ft); }
        SB();
        const bool one=ct&&c>=4;
        f32x4 a[3];
        #pragma unroll
        for(int j=0;j<3;j++) a[j]=f32x4{0,0,0,0};
        #pragma unroll
        for(int w=0;w<4;w++){
          const u32x4 vv={one?0x3f803f80u:vf[w][0][0],one?0x3f803f80u:vf[w][0][1],one?0x3f803f80u:vf[w][1][0],one?0x3f803f80u:vf[w][1][1]};
          #pragma unroll
          for(int ft=0;ft<3;ft++) a[ft]=MFMA(cat(ph[w][ft][0],ph[w][ft][1]),asb(vv),a[ft]);
        }
        if(ldr) WAITV(0);
        #pragma unroll
        for(int ft=0;ft<3;ft++) st128(R,nos4(__builtin_bit_cast(u32x4,a[ft])),ad3[ft],0);
      }
      WAITL0(); BAR();
    }
  }
  if(__builtin_expect(vi>=24,0)){
      const int sid=(vi-24)*8+xc, tid=threadIdx.x;
      BAR();
      { const int hr=sid>>3, pc=(sid&7)*6+wid;
        if(wid<6&&pc<45){
          const int ch=pc*16+c, cp=ch>=360?ch-360:ch;
          const unsigned vo_=((cp>=256&&cp<320&&(cp&15)>=12) || cp>=358)? OOB : ch*16+g*11520;
          u32x4 v[4];
          #pragma nounroll
          for(unsigned sp=0;sp<SPIN;sp++){ CFENCE();
            #pragma unroll
            for(int k=0;k<4;k++) v[k]=ld128(R,vo_,WS_PART+(hr*16+4*k)*11520);
            if(__all(mxu(mxu(mx4(v[0]),mx4(v[1])),mxu(mx4(v[2]),mx4(v[3])))!=SENT)) break;
            __builtin_amdgcn_s_sleep(HOP1SLEEP);
          }
          f32x4 s=(__builtin_bit_cast(f32x4,v[0])+__builtin_bit_cast(f32x4,v[1]))+(__builtin_bit_cast(f32x4,v[2])+__builtin_bit_cast(f32x4,v[3]));
          #pragma unroll
          for(int i=0;i<4;i++){
            auto a=__builtin_amdgcn_permlane32_swap(__float_as_uint(s[i]),__float_as_uint(s[i]),false,false);
            const float t=__uint_as_float(a[0])+__uint_as_float(a[1]);
            auto d=__builtin_amdgcn_permlane16_swap(__float_as_uint(t),__float_as_uint(t),false,false);
            s[i]=__uint_as_float(d[0])+__uint_as_float(d[1]); }
          st64(R,u32x2{nos(pk2(s[0],s[1])),nos(pk2(s[2],s[3]))},lane<16?lane*8:OOB,WS_KVF+hr*5760+pc*128);
        } }
      BAR();
      if(wid==7) codepf(vi-24);
      if(pt<2304) pacc^=*(const unsigned*)((const char*)wo+(2880+pt)*128);
      #pragma unroll
      for(int i=PF1A;i<PF1B;i++) if(i<PF1B-QSKIP||vi>=27) pf1(i);
      auto pf2=[&](int id){ id=id<18432?id:0; pacc^=*(const unsigned*)((const char*)w2+(size_t)(kq*1152+(id>>4))*(S*2)+(xc&1)*2048+(id&15)*128); };
      asm volatile(""::"v"(pacc));
      if(vi>=27){
        auto xclr=[&](){
          #pragma nounroll
          for(int i=(sid-24)*8+wid;i<1024;i+=320) if(((i>>2)&3)!=(i&3)){
            #pragma unroll
            for(int k=0;k<9;k++) st128(R,sent4,lane*16+k*1024,WS_X+i*9216); }
          WAITV(0); BAR();
          if(wid==0) set_flag(WS_XF+(sid-24)*128); };
        {
          if(wid==0) wait_flags(lane==0?0:OOB,WS_HF);
          BAR();
          const u64 t0=__builtin_amdgcn_s_memrealtime();
          xclr();
          while(__builtin_amdgcn_s_memrealtime()-t0<(u64)LATE) __builtin_amdgcn_s_sleep(8);
        }
        #pragma unroll
        for(int i=0;i<PF2N;i++) pf2(tid+(vi-27)*512+i*2560);
        asm volatile(""::"v"(pacc));
        if(vi==31){ codepf(wid); WAITV(0); }
      }
  } else {
    const bf16x4 z4=__builtin_bit_cast(bf16x4,u32x2{0,0});
    bf16x4 wr[36]; f32x4 acc[2][3]; int adB[3];
    if(!ldr){
      #pragma unroll
      for(int j=0;j<3;j++) adB[j]=(4*g+q4)*384+(((3*lw+j)^(((4*g+q4)>>1)&3))*32)+8*p;
      #pragma unroll
      for(int k=0;k<6;k++)
        #pragma unroll
        for(int m=0;m<6;m++) wr[k*6+m]=ldsT(sm+adB[m>>1]+(32*k+16*(m&1))*384);
      const unsigned one=(c&1)?0x3f800000u:0x3f80u; const int dw=(c>>1)&3;
      const bool il=g==(c>>3), ih=g==2+(c>>3);
      const u32x4 Ilo={il&&dw==0?one:0u,il&&dw==1?one:0u,il&&dw==2?one:0u,il&&dw==3?one:0u}, Ihi={ih&&dw==0?one:0u,ih&&dw==1?one:0u,ih&&dw==2?one:0u,ih&&dw==3?one:0u};
      const f32x4 zf={0,0,0,0};
      #pragma unroll
      for(int t=0;t<2;t++){ acc[t][0]=MFMA(asb(xa[t][0]),asb(Ilo),zf); acc[t][1]=MFMA(asb(xa[t][0]),asb(Ihi),zf); acc[t][2]=MFMA(asb(xa[t][1]),asb(Ihi),zf); }
      #pragma unroll
      for(int t=0;t<2;t++)
        #pragma unroll
        for(int j=0;j<3;j++) PIN(acc[t][j]);
      #pragma unroll
      for(int k=0;k<36;k++) PIN(wr[k]);
    }
    {
      const int g8=lw, hf=ldr?1:0, w8=2*g8+hf;
      const int gp=c>>2, r=c&3;
      const unsigned row1=(r==0)?gp:(r==1?4:5);
      const bool hs1=(r==0)?(gp>=2):(r==2);
      unsigned v2=(g*8+row1)*16, vt=(g&1)*2880+2560+c*16, vz=(g&1)*2880+2816+row1*8;
      int oa[2],ob[2],ox[2];
      #pragma unroll
      for(int pi=0;pi<2;pi++){
        const int P=2*w8+pi, ca=9*P+g, cx=9*P+4, cq=9*P+5+g;
        oa[pi]=P2_O+(ca>>3)*1024+((ca&3)*16+c)*16+((ca>>2)&1)*8;
        ob[pi]=P2_O+(cq>>3)*1024+((cq&3)*16+c)*16+((cq>>2)&1)*8;
        ox[pi]=P2_O+(cx>>3)*1024+((cx&3)*16+c)*16+((cx>>2)&1)*8+2*g;
        PIN(oa[pi]); PIN(ob[pi]); PIN(ox[pi]); }
      PIN(v2); PIN(vt); PIN(vz);
      wait_flags(lane==0?0:OOB,WS_QF+(((tb>>2)*4+g8)*8+(hf?0:4)+(tb&3))*128);
      u32x4 ph[2][5];
      #pragma unroll
      for(int t=0;t<2;t++){ const unsigned src=WS_PQ+((2*tb+t)*4+g8)*9216;
        #pragma unroll
        for(int s=0;s<4;s++) ph[t][s]=ld128(R,lane*16,src+(4*hf+s)*1024);
        ph[t][4]=ld128(R,(g>>1)==hf?lane*16:OOB,src+8192); }
      if(ldr){ ldwo(3,3); ldwo(4,4); }
      BAR();
      if(ii>=24) __builtin_amdgcn_s_sleep(KINIT);
      u32x4 k0[2],k1[2],k2[2],tt; u32x2 tz;
      #pragma nounroll
      for(unsigned sp=0;sp<SPIN;sp++){ CFENCE();
        const unsigned kp=WS_KVF+w8*5760;
        #pragma unroll
        for(int pi=0;pi<2;pi++){ k0[pi]=ld128(R,lane*16,kp+pi*2880); k1[pi]=ld128(R,lane*16,kp+pi*2880+1024); k2[pi]=ld128(R,v2,kp+pi*2880+2048); }
        tt=ld128(R,vt,kp); tz=ld64(R,vz,kp);
        unsigned m=mxu(mxu(mx4(tt),tz[0]),tz[1]);
        #pragma unroll
        for(int pi=0;pi<2;pi++) m=mxu(m,mxu(mxu(mx4(k0[pi]),mx4(k1[pi])),mx4(k2[pi])));
        if(__all(m!=SENT)) break;
        __builtin_amdgcn_s_sleep(HOP2SLEEP);
      }
      WAITV(0);
      const f32x4 zf={0,0,0,0};
      #pragma unroll
      for(int pi=0;pi<2;pi++){
        const bool act=(g==2*hf+pi), lo=r!=3&&!hs1, hi=r!=3&&hs1; const u32x4 z={0,0,0,0};
        const u32x4 F4=lo?k2[pi]:z, F5=hi?k2[pi]:z;
        const u32x4 F1={act?tt[0]:0u,act?tt[1]:0u,0u,0u}, F3={0u,0u,act?tt[2]:0u,act?tt[3]:0u};
        const u32x4 F6={act&&lo?tz[0]:0u,act&&lo?tz[1]:0u,act&&hi?tz[0]:0u,act&&hi?tz[1]:0u};
        #pragma unroll
        for(int t=0;t<2;t++){
          f32x4 d0=MFMA(asb(k0[pi]),asb(ph[t][2*pi]),zf), d2=MFMA(asb(k1[pi]),asb(ph[t][2*pi+1]),zf), d1=MFMA(asb(F4),asb(ph[t][2*pi]),zf);
          d1=MFMA(asb(F5),asb(ph[t][2*pi+1]),d1); d1=MFMA(asb(F6),asb(ph[t][4]),d1);
          d0=MFMA(asb(F1),asb(ph[t][4]),d0); d2=MFMA(asb(F3),asb(ph[t][4]),d2);
          float ra=__builtin_amdgcn_rcpf(d1[1]+1e-6f), rb=__builtin_amdgcn_rcpf(d1[2]+1e-6f);
          *(bf16x4*)(sm+t*18432+oa[pi])=pk4v(d0*ra);
          *(bf16x4*)(sm+t*18432+ob[pi])=pk4v(d2*rb);
          *(u16*)(sm+t*18432+ox[pi])=(u16)pk2(d1[0]*(g<2?ra:rb),0.f);
        }
      }
      WAITL0(); BAR();
    }
    if(ldr){
      __builtin_amdgcn_s_setprio(3);
      ldwo(5,0); ldwo(6,1); ldwo(7,2);
      WAITV(6); BAR(); ldwo(8,3);
      WAITV(6); BAR();
      WAITV(0); BAR();
      ldb3(0,0); ldb3(1,1);
      BAR();
    } else {
      bf16x8 af[2][2]; bf16x4 wf[2][6];
      const char* so=sm+P2_O+lane*16;
      auto kst=[](int n){ constexpr int ord[9]={3,4,0,1,2,5,6,7,8}; return ord[n>>1]*2+(n&1); };
      auto rd=[&](int n,int m){
        const int k=kst(n), st=k>>1, slot=st<5?st:(st==8?3:st-5);
        if(m<2) af[n&1][m]=ldsA(so+m*18432+k*1024);
        if(st>2) wf[n&1][m]=ldsT(sm+slot*P2_STB+adB[m>>1]+(32*(k&1)+16*(m&1))*384);
      };
      #pragma unroll
      for(int m=0;m<6;m++) rd(0,m);
      SB();
      #pragma unroll
      for(int n=0;n<18;n++){
        if(n==9||n==13||n==15){ BAR(); SB(); }
        const int k=kst(n);
        #pragma unroll
        for(int m=0;m<6;m++){
          const int j=m>>1,t=m&1;
          acc[t][j]=MFMA(af[n&1][t],k<6?cat(wr[(k<6?k:0)*6+2*j],wr[(k<6?k:0)*6+2*j+1]):cat(wf[n&1][2*j],wf[n&1][2*j+1]),acc[t][j]);
          SB();
          if(n<17) rd(n+1,m);
          SB();
        }
      }
      float s1[8],s2[8]; u32x4 hp[3];
      #pragma unroll
      for(int i=0;i<8;i++){ s1[i]=0; s2[i]=0; }
      #pragma unroll
      for(int j=0;j<3;j++)
        #pragma unroll
        for(int t=0;t<2;t++){
          hp[j][2*t]=pk2(acc[t][j][0],acc[t][j][1]); hp[j][2*t+1]=pk2(acc[t][j][2],acc[t][j][3]);
          acc[t][j]=f32x4{lo2f(hp[j][2*t]),hi2f(hp[j][2*t]),lo2f(hp[j][2*t+1]),hi2f(hp[j][2*t+1])};
          #pragma unroll
          for(int r=0;r<4;r++){ s1[4*t+r]+=acc[t][j][r]; s2[4*t+r]=fmaf(acc[t][j][r],acc[t][j][r],s2[4*t+r]); }
        }
      {
        float v[16];
        #pragma unroll
        for(int i=0;i<8;i++){ v[2*i]=s1[i]; v[2*i+1]=s2[i]; }
        #pragma unroll
        for(int i=0;i<8;i++){ const bool u=c&8; float k=u?v[i+8]:v[i], o=u?v[i]:v[i+8]; v[i]=k+dppmov<0x128>(o); }
        #pragma unroll
        for(int i=0;i<4;i++){ const bool u=c&4; float k=u?v[i+4]:v[i], o=u?v[i]:v[i+4]; v[i]=k+dppmov<0x1B>(dppmov<0x141>(o)); }
        #pragma unroll
        for(int i=0;i<2;i++){ const bool u=c&2; float k=u?v[i+2]:v[i], o=u?v[i]:v[i+2]; v[i]=k+dppmov<0x4E>(o); }
        { const bool u=c&1; float k=u?v[1]:v[0], o=u?v[0]:v[1]; v[0]=k+dppmov<0xB1>(o); }
        st32(R,nos(__float_as_uint(v[0])),((c>>3)*32+g*8+(c&7))*4,WS_SX+(tb*12+cb*4+lw)*256);
      }
      {
        u32x2 a[6];
        __builtin_amdgcn_s_sleep(S2INIT);
        #pragma nounroll
        for(unsigned sp=0;sp<SPIN;sp++){ CFENCE();
          unsigned m=0;
          #pragma unroll
          for(int k=0;k<6;k++) a[k]=ld64(R,lane*8,WS_SX+tb*3072+k*512);
          #pragma unroll
          for(int k=0;k<6;k++) m=mxu(m,mxu(a[k][0],a[k][1]));
          if(__all(m!=SENT)) break;
          __builtin_amdgcn_s_sleep(S2SLEEP);
        }
        f32x2 t=__builtin_bit_cast(f32x2,a[0]);
        #pragma unroll
        for(int k=1;k<6;k++) t+=__builtin_bit_cast(f32x2,a[k]);
        auto w0_=__builtin_amdgcn_permlane32_swap(__float_as_uint(t[0]),__float_as_uint(t[0]),false,false);
        auto w1_=__builtin_amdgcn_permlane32_swap(__float_as_uint(t[1]),__float_as_uint(t[1]),false,false);
        const float mu=(__uint_as_float(w0_[0])+__uint_as_float(w0_[1]))*(1.f/576.f), ex2=(__uint_as_float(w1_[0])+__uint_as_float(w1_[1]))*(1.f/576.f);
        const float var=fmaxf(ex2-mu*mu,0.f);
        const float r1=__builtin_amdgcn_rsqf(var+1e-5f);
        const float r2=__builtin_amdgcn_rsqf(var*r1*r1+1e-5f);
        *(float*)(sm+P2_FS+lw*256+lane*4)=lane<32? -mu*(r1*r2) : r1*r2;
        CFENCE();
      }
      f32x4 mu[2],rs[2];
      #pragma unroll
      for(int t=0;t<2;t++){ mu[t]=*(const f32x4*)(sm+P2_FS+lw*256+(16*t+4*g)*4); rs[t]=*(const f32x4*)(sm+P2_FS+lw*256+128+(16*t+4*g)*4); }
      #pragma unroll
      for(int j=0;j<3;j++){
        const int T=12*cb+3*lw+j, m6=T/6;
        st128(R,cvt8(acc[0][j]*rs[0]+mu[0],acc[1][j]*rs[1]+mu[1]),lane*16,WS_HN+((m6*64+tb)*6+(T-6*m6))*1024);
      }
      WAITV(0); BAR();
      if(lw==0) set_flag(WS_HF+(cb*64+tb)*128);
      #pragma unroll
      for(int j=0;j<3;j++) st128(R,hp[j],lane*16,WS_HT+(tb*36+12*cb+3*lw+j)*1024);
    }

  }
  if(vi<27){
    constexpr int PD=P3_ST-2, NT=32;
    if(ldr){
      __builtin_amdgcn_s_setprio(3);
      const u32x4 rA=mksrd(ws+WS_HN+(size_t)mt*64*6*1024, 64*6*1024);
      const int va=lane*16+lw*3072;
      auto lda=[&](int t,int slot){ glds3<16>(rA,smb+slot*P3_STB+16384+lw*3072,(P3A_T)*12288,va,va,va); };
      if(vi>=24){ ldb3(0,0); ldb3(1,1); }
      ldb3(2,2);
      int tst; { const int t=lane; tst=P3A_T; PIN(tst); }
      if(lw==0){
        if(vi>=24) wait_flags(lane==0?0:OOB,WS_HF); else { if(vi>=16) __builtin_amdgcn_s_sleep(S3EXT); __builtin_amdgcn_s_sleep(S3INIT); }
        wait_flags(lane*128,WS_HF+(mt>>1)*8192); }
      BAR();
      lda(0,0); lda(1,1); lda(2,2);
      WAITV(6); BAR();
      lda(3,3); ldb3(3,3);
      WAITV(10); BAR();
      lda(4,4); ldb3(4,4);
      { const u32x4 rB=mksrd((const char*)w1+nt*256, S*F*2-nt*256);
        const int v0=vb3(0),v1=vb3(1),v2=vb3(2),v3=vb3(3);
        unsigned lb=smb+lw*4096; const unsigned lend=lb+P3_ST*P3_STB;
        __builtin_amdgcn_s_setprio(LPRIO);
        #pragma nounroll
        for(int t=5;t<NT+PD;t++){
          const int s_=__builtin_amdgcn_readlane(tst,t); int sa=s_*12288, sb=s_*64*(F*2); unsigned la=lb+16384-lw*1024;
          PINS(sa); PINS(sb); PINS(la);
          WAITV(14); BAR();
          WAITV(CAP-3);
          glds3<16>(rA,la,sa,va,va,va);
          WAITV(CAP-4);
          glds4<0>(rB,lb,sb,v0,v1,v2,v3);
          lb+=P3_STB; if(lb==lend) lb-=P3_ST*P3_STB;
        } }
      WAITV(0);
      BAR();
      if(b==0&&lw==0)st64(R,u32x2{ev[0]+1u,ev[1]+(ev[0]==0xFFFFFFFFu?1u:0u)},lane==0?0:OOB,WS_E);

    } else {
      const int xs=4*(g&1)+q4;
      int adB[2];
      #pragma unroll
      for(int j=0;j<2;j++) adB[j]=smb+(4*g+q4)*256+(((2*lw+j)^xs)*32)+p*8;
      int adA=smb+16384+lane*16;
      f32x4 acc[6][2];
      { u32x2 bv[2];
        #pragma unroll
        for(int j=0;j<2;j++) bv[j]=*(const u32x2*)(b1+nt*128+lw*32+16*j+4*g);
        #pragma unroll
        for(int j=0;j<2;j++){ f32x4 bb={lo2f(bv[j][0]),hi2f(bv[j][0]),lo2f(bv[j][1]),hi2f(bv[j][1])};
          #pragma unroll
          for(int i=0;i<6;i++) acc[i][j]=bb; } }
      bf16x8 a0[6],a1[6]; bf16x4 w0[4],w1f[4];
      #pragma unroll
      for(int i=0;i<6;i++) a1[i]=asb(u32x4{0,0,0,0});
      #pragma unroll
      for(int j=0;j<4;j++) w1f[j]=__builtin_bit_cast(bf16x4,u32x2{0,0});
      int slot=0,dlt=0,rem=NT;
      auto half=[&](bf16x4 (&wc)[4], bf16x8 (&ac)[6], bf16x4 (&wx)[4], bf16x8 (&ax)[6], int ks){
        #pragma unroll
        for(int m=0;m<12;m++){
          const int i=m>>1,j=m&1;
          if(m==0) __builtin_amdgcn_s_waitcnt(0xC07F|(3<<8));
          if(m==6) __builtin_amdgcn_s_waitcnt(0xC07F|(6<<8));
          SB();
          mfma_ip(acc[i][j],cat(wc[2*j],wc[2*j+1]),ac[i]);
          SB();
          if(m<4) wx[m]=ldsTi(adB[m>>1]+ks*8192+(m&1)*4096);
          else if(m<10) ax[m-4]=ldsAi(adA+(ks*6+m-4)*1024);
          else if(ks){ if(m==10){ adA+=dlt; adB[0]+=dlt; adB[1]+=dlt; PIN(adA); PIN(adB[0]); PIN(adB[1]); } }
          else if(m==10){ dlt=slot==P3_ST-1? -(P3_ST-1)*P3_STB : P3_STB; PINS(dlt); }
          else { slot=slot==P3_ST-1?0:slot+1; rem--; PINS(slot); PINS(rem); }
          SB();
        }
      };
      #pragma unroll
      for(int i=0;i<6;i++){ PIN(acc[i][0]); PIN(acc[i][1]); PIN(a1[i]); }
      __builtin_amdgcn_s_waitcnt(0xC07F);
      BAR();
      #pragma nounroll
      do{
        BAR();
        SB();
        half(w1f,a1,w0,a0,0);
        half(w0,a0,w1f,a1,1);
      }while(rem);
      #pragma unroll
      for(int i=0;i<6;i++)
        #pragma unroll
        for(int j=0;j<2;j++) acc[i][j]=MFMA(cat(w1f[2*j],w1f[2*j+1]),a1[i],acc[i][j]);
      WAITL0(); BAR();
      #pragma unroll
      for(int i=0;i<6;i++){
        const int T=mt*6+i, m9=T/9;
        st128(R,u32x4{gelu2(acc[i][0][0],acc[i][0][1]),gelu2(acc[i][0][2],acc[i][0][3]),gelu2(acc[i][1][0],acc[i][1][1]),gelu2(acc[i][1][2],acc[i][1][3])},lane*16,WS_U+((m9*144+nt*4+lw)*9+(T-9*m9))*1024);
      }
      WAITV(0);
    }
  }
  {
    constexpr int NT=18;
    const int s0=nb*128+kq*32;
    if(ldr){
      __builtin_amdgcn_s_setprio(3);
      const u32x4 rA=mksrd(ws+WS_U+((size_t)mb*144+kq*36)*9*1024, 36*9*1024);
      const u32x4 rB=mksrd((const char*)w2+(size_t)kq*1152*(S*2)+nb*256, 1152*S*2-nb*256);
      int vb[4];
      #pragma unroll
      for(int q=0;q<4;q++) vb[q]=(lw*16+q*4+g)*(S*2)-q*1024+(((c>>1)^(4*(q&1)+g))*32)+(c&1)*16;
      const int va=lane*16+lw*4096, vx=lane*16+(16+lw)*1024;
      u64 rdy=0;
      auto chk=[&](int t){ const u64 need=3ull<<(t&~1); if((rdy&need)!=need) rdy|=wait_mask(lane<18?((lane>>1)*6+(lane&1))*128:OOB,WS_UF+(54*kq+(9*mb)/6)*128,need&~rdy); };
      auto lda=[&](int t_,int slot){ const int t=kperm(t_,(mb+nb)&3,18);
        chk(t);
        glds4<16>(rA,smb+slot*P4_STB+lw*4096,t*18432,va,va,va,va);
        glds_lt2<16>(lw,rA,smb+slot*P4_STB+(16+lw)*1024,vx,t*18432);
      };
      auto ldb=[&](int t_,int slot){ const int t=kperm(t_,(mb+nb)&3,18);
        glds4<0>(rB,smb+slot*P4_STB+18432+lw*4096,t*64*(S*2),vb[0],vb[1],vb[2],vb[3]);
      };
      ldb(0,0); ldb(1,1);
      const float bb0=b2f(b2[s0+c]), bb1=b2f(b2[s0+16+c]);
      BAR();
      { const int t0=kperm(0,(mb+nb)&3,18), t1=kperm(1,(mb+nb)&3,18); rdy=(3ull<<(t0&~1))|(3ull<<(t1&~1));
        wait_flags(((rdy>>lane)&1)?((lane>>1)*6+(lane&1))*128:OOB,WS_UF+(54*kq+(9*mb)/6)*128); }
      lda(0,0); lda(1,1);
      waitv2<5,4>(lw); BAR(); lda(2,2); ldb(2,2);
      __builtin_amdgcn_s_setprio(LPRIO);
      #pragma nounroll
      for(int n=3;n<NT;n++){
        const int t=kperm(n,(mb+nb)&3,18);
        int sa=t*18432, sb=t*64*(S*2); unsigned la=smb+(n&3)*P4_STB+lw*4096, lx=la+16384-lw*3072, lb=la+18432;
        PINS(sa); PINS(sb); PINS(la); PINS(lx); PINS(lb);
        waitv2<9,8>(lw); BAR();
        chk(t);
        glds4<16>(rA,la,sa,va,va,va,va);
        glds4<0>(rB,lb,sb,vb[0],vb[1],vb[2],vb[3]); glds_lt2<16>(lw,rA,lx,vx,sa);
      }
      waitv2<9,8>(lw); BAR();
      { const int tbh=nb*4+kq;
        wait_flags(lane<40? WS_XF+lane*128 : (lane<43? WS_UF+((tbh>>3)*27+(tbh&7)*3+lane-40)*128 : OOB),0); }
      auto itm=[&](int q){ return q<2? 2*lw+(mb&1)+q : (lw? 9 : ((mb&1)?0:8)); };
      const u32x4 rH=mksrd(ws+WS_HT+((size_t)(nb*4+kq)*36+mb*9)*1024, 9*1024);
      #pragma unroll
      for(int q=0;q<3;q++) glds<16>(rH,smb+P4_H+(q*4+lw)*1024,lane*16,itm(q)*1024);
      WAITV(3); BAR();
      WAITV(0);
      f32x4 hv[3][2];
      #pragma unroll
      for(int q=0;q<3;q++)
        #pragma unroll
        for(int n=0;n<2;n++){ u32x2 h=__builtin_bit_cast(u32x2,ldsT(sm+P4_H+(q*4+lw)*1024+(p*16+4*g+q4)*16+n*8)); const float bb=n?bb1:bb0;
          hv[q][n]=f32x4{lo2f(h[0])+bb,hi2f(h[0])+bb,lo2f(h[1])+bb,hi2f(h[1])+bb}; PIN(hv[q][n]); }
      unsigned oo=((s0+(lane>>2))*D+mb*144+itm(0)*16)*2+p*16, oz=lw? OOB : ((s0+(lane>>1))*D+mb*144+itm(2)*16)*2+(lane&1)*16; PIN(oo); PIN(oz);
      u32x4 pv[3][3];
      auto ldp=[&](int z){ const int snd=z+(z>=kq?1:0);
        #pragma unroll
        for(int q=0;q<3;q++) pv[q][z]=ld128(R,(q<2||lw==0)?lane*16:OOB,WS_X+((tile*4+kq)*4+snd)*9216+itm(q)*1024); };
      #pragma unroll
      for(int q=0;q<3;q++)
        #pragma unroll
        for(int z=0;z<3;z++) pv[q][z]=sent4;
      { unsigned got=0; const unsigned ho=lane<3?(lane+(lane>=kq?1:0))*128:OOB;
        #pragma nounroll
        for(unsigned sp=0;sp<SPIN;sp++){ CFENCE();
          const unsigned nw=(unsigned)__ballot(ld32(R,ho,WS_XH+(tile*4+kq)*512)==1u)&~got;
          CFENCE();
          if(nw&1) ldp(0);
          if(nw&2) ldp(1);
          if(nw&4) ldp(2);
          got|=nw;
          if(got==7) break;
          __builtin_amdgcn_s_sleep(SL5);
        } }
      BAR();
      #pragma unroll
      for(int q=0;q<3;q++)
        #pragma unroll
        for(int n=0;n<2;n++){ const int it=(q<2||lw==0)?itm(q):0; hv[q][n]+=*(const f32x4*)(sm+2*P4_STB+(it*2+n)*1024+lane*16); PIN(hv[q][n]); }
      #pragma nounroll
      for(unsigned sp=0;sp<(1u<<14);sp++){
        unsigned m=0;
        #pragma unroll
        for(int q=0;q<3;q++)
          #pragma unroll
          for(int z=0;z<3;z++) m=mxu(m,mx4(pv[q][z]));
        if(__all(m!=SENT)) break;
        CFENCE(); ldp(0); ldp(1); ldp(2);
      }
      const rsrc_t RO=__builtin_amdgcn_make_buffer_rsrc(out,0,S*D*2,0x00020000);
      char* tw=sm+3*P4_STB+lw*4096;
      #pragma unroll
      for(int q=0;q<3;q++){
        f32x4 v0=hv[q][0],v1=hv[q][1];
        #pragma unroll
        for(int z=0;z<3;z++){ u32x4 w=pv[q][z];
          v0+=f32x4{lo2f(w[0]),hi2f(w[0]),lo2f(w[1]),hi2f(w[1])}; v1+=f32x4{lo2f(w[2]),hi2f(w[2]),lo2f(w[3]),hi2f(w[3])}; }
        *(bf16x4*)(tw+c*112+q*32+g*8)=pk4v(v0); *(bf16x4*)(tw+(16+c)*112+q*32+g*8)=pk4v(v1);
      }
      CFENCE();
      const u32x4 o0=*(const u32x4*)(tw+(lane>>2)*112+p*16), o1=*(const u32x4*)(tw+(16+(lane>>2))*112+p*16), o2=*(const u32x4*)(tw+(lane>>1)*112+64+(lane&1)*16);
      __builtin_amdgcn_raw_buffer_store_b128(o0,RO,oo,0,OAUX); __builtin_amdgcn_raw_buffer_store_b128(o1,RO,oo+32*D,0,OAUX);
      __builtin_amdgcn_raw_buffer_store_b128(o2,RO,oz,0,OAUX);
    } else {
      BAR();
      if(lw==0&&vi<27) set_flag(WS_UF+L3*128);
      const int xs=4*(g&1)+q4;
      int adB[2];
      #pragma unroll
      for(int j=0;j<2;j++) adB[j]=18432+(4*g+q4)*256+(((2*lw+j)^xs)*32)+p*8;
      f32x4 acc[9][2];
      #pragma unroll
      for(int i=0;i<9;i++){ acc[i][0]=f32x4{0,0,0,0}; acc[i][1]=f32x4{0,0,0,0}; }
      bf16x8 a0[9],a1[9]; bf16x4 w0[4],w1f[4];
      #pragma unroll
      for(int i=0;i<9;i++) a1[i]=asb(u32x4{0,0,0,0});
      #pragma unroll
      for(int j=0;j<4;j++) w1f[j]=__builtin_bit_cast(bf16x4,u32x2{0,0});
      int adA=smb+lane*16, dlt=0, cnt=0; adB[0]+=smb; adB[1]+=smb;
      auto half=[&](bf16x4 (&wc)[4], bf16x8 (&ac)[9], bf16x4 (&wx)[4], bf16x8 (&ax)[9], int ks){
        #pragma unroll
        for(int m=0;m<18;m++){
          const int i=m>>1,j=m&1;
          if(m==0) __builtin_amdgcn_s_waitcnt(0xC07F|(P4W<<8));
          if(m==10) __builtin_amdgcn_s_waitcnt(0xC07F|(10<<8));
          SB();
          mfma_ip(acc[i][j],ac[i],cat(wc[2*j],wc[2*j+1]));
          SB();
          if(m<4) wx[m]=ldsTi(adB[m>>1]+ks*8192+(m&1)*4096);
          else if(m<13) ax[m-4]=ldsAi(adA+(ks*9+m-4)*1024);
          else if(ks){ if(m==13){ adA+=dlt; adB[0]+=dlt; PIN(adA); PIN(adB[0]); } if(m==14){ adB[1]+=dlt; PIN(adB[1]); } }
          else if(m==13){ dlt=(cnt&3)==3? -3*P4_STB : P4_STB; PINS(dlt); }
          else if(m==14){ cnt++; PINS(cnt); }
          SB();
        }
      };
      #pragma unroll
      for(int i=0;i<9;i++){ PIN(acc[i][0]); PIN(acc[i][1]); PIN(a1[i]); }
      #pragma unroll
      for(int j=0;j<4;j++) PIN(w1f[j]);
      __builtin_amdgcn_s_waitcnt(0xC07F);
      #pragma nounroll
      do{
        BAR();
        SB();
        half(w1f,a1,w0,a0,0);
        half(w0,a0,w1f,a1,1);
      }while(cnt!=NT-1);
      BAR(); SB();
      half(w1f,a1,w0,a0,0);
      auto last=[&](auto SEND){
        const unsigned xo=lane*16+WS_X+((tile*4+lw)*4+kq)*9216;
        #pragma unroll
        for(int i=0;i<13;i++){
          if(i<9){
            acc[i][0]=MFMA(a0[i],cat(w0[0],w0[1]),acc[i][0]); SB();
            if(i<2) w1f[2*i]=ldsTi(adB[i]+8192);
            if(i<2) w1f[2*i+1]=ldsTi(adB[i]+8192+4096);
            SB();
            acc[i][1]=MFMA(a0[i],cat(w0[2],w0[3]),acc[i][1]); SB();
            a1[i]=ldsAi(adA+(9+i)*1024); SB(); }
          if(i>=3&&i<12){ acc[i-3][0]=MFMA(a1[i-3],cat(w1f[0],w1f[1]),acc[i-3][0]); acc[i-3][1]=MFMA(a1[i-3],cat(w1f[2],w1f[3]),acc[i-3][1]); SB(); }
          if(decltype(SEND)::value&&i>=4){ st128(R,cvt8(acc[i-4][0],acc[i-4][1]),xo+(i-4)*1024,0); SB(); }
        } };
      if(lw!=kq){
        last(Int<1>{});
        set_flag(WS_XH+((tile*4+lw)*4+kq)*128);
      } else {
        last(Int<0>{});
        #pragma unroll
        for(int it=0;it<9;it++){ *(f32x4*)(sm+2*P4_STB+(it*2)*1024+lane*16)=acc[it][0]; *(f32x4*)(sm+2*P4_STB+(it*2+1)*1024+lane*16)=acc[it][1]; }
        WAITL0();
      }
      BAR();
    }
  }
}

extern "C" void kernel_launch(void* const* d_in, const int* in_sizes, int n_in, void* d_out, int out_size, void* d_ws, size_t ws_size, hipStream_t stream){
  (void)hipFuncSetAttribute((const void*)fused, hipFuncAttributeMaxDynamicSharedMemorySize, LDS_BYTES);
  fused<<<256,512,LDS_BYTES,stream>>>((const u16*)d_in[0],(const u16*)d_in[1],(const u16*)d_in[2],(const u16*)d_in[3],(const u16*)d_in[5],(const u16*)d_in[6],(const u16*)d_in[7],(const u16*)d_in[8],(u16*)d_out,(char*)d_ws);
}
```
